```python
import torch

def _softmax(x, axis=-1):
    return torch.softmax(x, dim=axis)
def _gelu(x):
    return torch.nn.functional.gelu(x, approximate="tanh")
def _silu(x):
    return torch.nn.functional.silu(x)
def _sigmoid(x):
    return torch.sigmoid(x)
def _layernorm(x, axis=-1, eps=1e-5):
    m = x.mean(dim=axis, keepdim=True)
    v = x.var(dim=axis, keepdim=True, unbiased=False)
    return (x - m) / torch.sqrt(v + eps)
def _scatter_add(tgt, idx, src, axis=0):
    return tgt.clone().index_add_(axis, idx, src)
def _segment_sum(src, seg, n):
    out = torch.zeros((n,) + src.shape[1:], dtype=src.dtype)
    return out.index_add_(0, seg, src)
def _pad(x, pw):
    flat = tuple(v for pair in reversed(pw) for v in pair)
    return torch.nn.functional.pad(x, flat)

kernel_name = 'recurrent_scan__mha_block_cached__glu_gated_swiglu__decode'

def setup_inputs():

    _w = (lambda a: (lambda t: t.to(torch.bfloat16) if t.is_floating_point() else t)(torch.as_tensor(a)))
    def _A_setup():
        B, Sq, D = 1, 1, 640
        H, Sk, Dh = 32, 32768, 192
        return {
            "x": _w((lambda *s: torch.randn(*s, dtype=torch.bfloat16) * 0.2)(B, Sq, D)),
            "k_cache": _w((lambda *s: torch.randn(*s, dtype=torch.bfloat16) * 1.6)(B, H, Sk, Dh)),
            "v_cache": _w((lambda *s: torch.randn(*s, dtype=torch.bfloat16) * 1.6)(B, H, Sk, Dh)),
            "wq": _w((lambda *s: torch.randn(*s, dtype=torch.bfloat16) * 1.6)(D, H * Dh) * (D ** -0.5)),
            "wo": _w((lambda *s: torch.randn(*s, dtype=torch.bfloat16) * 1.6)(H * Dh, D) * ((H * Dh) ** -0.5)),
        }
    _A_in = _A_setup()

    def _B_setup():
        B, S, D, F = 1, 1, 640, 1536
        return {
            "x": _w((lambda *s: torch.randn(*s, dtype=torch.bfloat16) * 0.2)(B, S, D)),
            "w1": _w((lambda *s: torch.randn(*s, dtype=torch.bfloat16) * 0.8)(D, F) * (2.0 / D) ** 0.5),
            "w3": _w((lambda *s: torch.randn(*s, dtype=torch.bfloat16) * 0.8)(D, F) * (2.0 / D) ** 0.5),
            "w2": _w((lambda *s: torch.randn(*s, dtype=torch.bfloat16) * 0.8)(F, D) * (2.0 / F) ** 0.5),
        }
    _B_in = _B_setup()

    return {
        "A_x": _A_in["x"],
        "A_k_cache": _A_in["k_cache"],
        "A_v_cache": _A_in["v_cache"],
        "A_wq": _A_in["wq"],
        "A_wo": _A_in["wo"],
        "B_x": _B_in["x"],
        "B_w1": _B_in["w1"],
        "B_w3": _B_in["w3"],
        "B_w2": _B_in["w2"],
    }

def reference(A_x, A_k_cache, A_v_cache, A_wq, A_wo, B_x, B_w1, B_w3, B_w2):
    def _A_ref(x, k_cache, v_cache, wq, wo):
        B, Sq, D = x.shape
        H, Dh = 32, 192
        q = (lambda x, *d: x.permute(*d))((x @ wq).reshape(B, Sq, H, Dh), 0, 2, 1, 3)
        scores = torch.einsum("bhqd,bhkd->bhqk", q, k_cache) * (Dh ** -0.5)
        p = _softmax(scores, axis=-1)
        o = torch.einsum("bhqk,bhkd->bhqd", p, v_cache)
        return (lambda x, *d: x.permute(*d))(o, 0, 2, 1, 3).reshape(B, Sq, H * Dh) @ wo

    def _B_ref(x, w1, w3, w2):
        gate = torch.einsum("bsd,df->bsf", x, w1)
        up = torch.einsum("bsd,df->bsf", x, w3)
        h = _silu(gate) * up
        out = torch.einsum("bsf,fd->bsd", h, w2)
        return out

    x = A_x
    xn = _layernorm(x, axis=-1)
    s = _A_ref(xn, A_k_cache, A_v_cache, A_wq, A_wo)
    g = _silu(_B_ref(xn, B_w1, B_w3, B_w2))
    return x + s * g
```

```cpp
#include <hip/hip_runtime.h>
#include <hip/hip_bf16.h>
#include <cstdint>

constexpr int DM = 640, NH = 32, SK = 32768, DH = 192, FF = 1536, HD = NH * DH;

#define K2_NSPLIT 16
#define K2_NW 8
#define K2_U 2
constexpr int NSPLIT = K2_NSPLIT, NW = K2_NW, U = K2_U;
constexpr int KPW = SK / NSPLIT / NW;
static_assert(KPW % (8 * U) == 0, "");

constexpr size_t WS_Q = 0;
constexpr size_t WS_H = 16384;
constexpr size_t WS_PML = 32768;
constexpr size_t WS_PO = 65536;
constexpr size_t WS_SP = WS_PO + (size_t)NH * NSPLIT * DH * 4;

using u32x4 = __attribute__((ext_vector_type(4))) unsigned;
using f32x4 = __attribute__((ext_vector_type(4))) float;
using bf16x2 = __attribute__((ext_vector_type(2))) __bf16;

__device__ __forceinline__ float bf2f(uint16_t b) { return __builtin_bit_cast(float, (unsigned)b << 16); }
__device__ __forceinline__ uint16_t f2bf(float f) {
  unsigned u = __builtin_bit_cast(unsigned, f);
  u += 0x7FFFu + ((u >> 16) & 1u);
  return (uint16_t)(u >> 16);
}
__device__ __forceinline__ float rbf(float f) { return bf2f(f2bf(f)); }
__device__ __forceinline__ float dot2(unsigned a, unsigned b, float acc) {
  return __builtin_amdgcn_fdot2_f32_bf16(__builtin_bit_cast(bf16x2, a), __builtin_bit_cast(bf16x2, b), acc, false);
}
template <int CTRL> __device__ __forceinline__ float dpp(float x) {
  return __builtin_bit_cast(float, __builtin_amdgcn_mov_dpp(__builtin_bit_cast(int, x), CTRL, 0xf, 0xf, true));
}
__device__ __forceinline__ float lo_f(unsigned w) { return __builtin_bit_cast(float, w << 16); }
__device__ __forceinline__ float hi_f(unsigned w) { return __builtin_bit_cast(float, w & 0xffff0000u); }

__global__ void __launch_bounds__(512) k1_proj(const uint16_t* __restrict__ x, const uint16_t* __restrict__ wq,
                                               const uint16_t* __restrict__ w1, const uint16_t* __restrict__ w3,
                                               uint16_t* __restrict__ q_out, float* __restrict__ h_out) {
  __shared__ float xn_s[DM];
  __shared__ float red[16];
  __shared__ float part[2][8][64];
  const int tid = threadIdx.x, lane = tid & 63, wave = tid >> 6, g = lane >> 3, i = lane & 7;
  const bool isq = blockIdx.x < 96;
  const int ncol = isq ? HD : FF;
  const int col0 = (isq ? blockIdx.x : blockIdx.x - 96) * 64;
  const uint16_t* wa = isq ? wq : w1;
  u32x4 wa_r[10], wb_r[10];
#pragma unroll
  for (int it = 0; it < 10; ++it) {
    const int r = wave * 80 + it * 8 + g;
    wa_r[it] = __builtin_nontemporal_load(reinterpret_cast<const u32x4*>(wa + (size_t)r * ncol + col0 + i * 8));
  }
  if (!isq) {
#pragma unroll
    for (int it = 0; it < 10; ++it) {
      const int r = wave * 80 + it * 8 + g;
      wb_r[it] = __builtin_nontemporal_load(reinterpret_cast<const u32x4*>(w3 + (size_t)r * ncol + col0 + i * 8));
    }
  }
  float xv0 = bf2f(x[tid]);
  float xv1 = (tid + 512 < DM) ? bf2f(x[tid + 512]) : 0.f;
  float s = xv0 + xv1;
#pragma unroll
  for (int o = 32; o >= 1; o >>= 1) s += __shfl_xor(s, o);
  if (lane == 0) red[wave] = s;
  __syncthreads();
  float tot = 0.f;
#pragma unroll
  for (int w = 0; w < 8; ++w) tot += red[w];
  const float mean = tot * (1.f / DM);
  float d0 = xv0 - mean, d1 = (tid + 512 < DM) ? xv1 - mean : 0.f;
  float s2 = d0 * d0 + d1 * d1;
#pragma unroll
  for (int o = 32; o >= 1; o >>= 1) s2 += __shfl_xor(s2, o);
  if (lane == 0) red[8 + wave] = s2;
  __syncthreads();
  float tot2 = 0.f;
#pragma unroll
  for (int w = 0; w < 8; ++w) tot2 += red[8 + w];
  const float mb = rbf(mean), vb = rbf(tot2 * (1.f / DM));
  const float den = rbf(sqrtf(rbf(vb + 1e-5f)));
  xn_s[tid] = rbf(rbf(xv0 - mb) / den);
  if (tid + 512 < DM) xn_s[tid + 512] = rbf(rbf(xv1 - mb) / den);
  __syncthreads();
  float acc[8], accb[8];
#pragma unroll
  for (int k = 0; k < 8; ++k) { acc[k] = 0.f; accb[k] = 0.f; }
#pragma unroll
  for (int it = 0; it < 10; ++it) {
    const float xv = xn_s[wave * 80 + it * 8 + g];
#pragma unroll
    for (int k = 0; k < 4; ++k) { acc[2 * k] = fmaf(xv, lo_f(wa_r[it][k]), acc[2 * k]); acc[2 * k + 1] = fmaf(xv, hi_f(wa_r[it][k]), acc[2 * k + 1]); }
    if (!isq) {
#pragma unroll
      for (int k = 0; k < 4; ++k) { accb[2 * k] = fmaf(xv, lo_f(wb_r[it][k]), accb[2 * k]); accb[2 * k + 1] = fmaf(xv, hi_f(wb_r[it][k]), accb[2 * k + 1]); }
    }
  }
#pragma unroll
  for (int k = 0; k < 8; ++k) {
    acc[k] += __shfl_xor(acc[k], 8); acc[k] += __shfl_xor(acc[k], 16); acc[k] += __shfl_xor(acc[k], 32);
    accb[k] += __shfl_xor(accb[k], 8); accb[k] += __shfl_xor(accb[k], 16); accb[k] += __shfl_xor(accb[k], 32);
  }
  if (g == 0) {
#pragma unroll
    for (int k = 0; k < 8; ++k) { part[0][wave][i * 8 + k] = acc[k]; part[1][wave][i * 8 + k] = accb[k]; }
  }
  __syncthreads();
  if (tid < 64) {
    float a = 0.f, b = 0.f;
#pragma unroll
    for (int w = 0; w < 8; ++w) { a += part[0][w][tid]; b += part[1][w][tid]; }
    if (isq) q_out[col0 + tid] = f2bf(a);
    else {
      const float gt = rbf(a), up = rbf(b);
      const float sl = rbf(gt / (1.f + __expf(-gt)));
      h_out[col0 + tid] = rbf(sl * up);
    }
  }
}

__global__ void __launch_bounds__(NW * 64) k2_attn(const uint16_t* __restrict__ q, const uint16_t* __restrict__ kc,
                                                   const uint16_t* __restrict__ vc, float* __restrict__ pml, float* __restrict__ po) {
  __shared__ __attribute__((aligned(16))) float mrg[NW][DH + 4];
  const int split = blockIdx.x, h = blockIdx.y;
  const int tid = threadIdx.x, lane = tid & 63, wave = tid >> 6, g = lane >> 3, i = lane & 7;
  constexpr float C2 = 0.07216878364870322f * 1.4426950408889634f;
  const size_t row0 = (size_t)h * SK + (size_t)(split * NW + wave) * KPW + g;
  const uint16_t* kp = kc + row0 * DH + i * 8;
  const uint16_t* vp = vc + row0 * DH + i * 8;
  u32x4 qv[3];
#pragma unroll
  for (int c = 0; c < 3; ++c) qv[c] = *reinterpret_cast<const u32x4*>(q + h * DH + (i + 8 * c) * 8);
  u32x4 Kn[U][3], Vn[U][3];
#define ISSUE(t_) do { _Pragma("unroll") for (int u_ = 0; u_ < U; ++u_) _Pragma("unroll") for (int c_ = 0; c_ < 3; ++c_) { \
      Kn[u_][c_] = __builtin_nontemporal_load(reinterpret_cast<const u32x4*>(kp + ((size_t)(t_) * 8 * U + u_ * 8) * DH + c_ * 64)); } \
    _Pragma("unroll") for (int u_ = 0; u_ < U; ++u_) _Pragma("unroll") for (int c_ = 0; c_ < 3; ++c_) { \
      Vn[u_][c_] = __builtin_nontemporal_load(reinterpret_cast<const u32x4*>(vp + ((size_t)(t_) * 8 * U + u_ * 8) * DH + c_ * 64)); } } while (0)
  float m = -INFINITY, l = 0.f, acc[24];
#pragma unroll
  for (int k = 0; k < 24; ++k) acc[k] = 0.f;
  constexpr int NT = KPW / (8 * U);
  ISSUE(0);
  for (int t = 0; t < NT; ++t) {
    u32x4 Kc[U][3], Vc[U][3];
#pragma unroll
    for (int u = 0; u < U; ++u)
#pragma unroll
      for (int c = 0; c < 3; ++c) { Kc[u][c] = Kn[u][c]; Vc[u][c] = Vn[u][c]; }
    if (t + 1 < NT) ISSUE(t + 1);
    float sc[U];
#pragma unroll
    for (int u = 0; u < U; ++u) {
      float d = 0.f;
#pragma unroll
      for (int c = 0; c < 3; ++c)
#pragma unroll
        for (int k = 0; k < 4; ++k) d = dot2(Kc[u][c][k], qv[c][k], d);
      d += dpp<0xB1>(d); d += dpp<0x4E>(d); d += dpp<0x141>(d);
      sc[u] = d * C2;
    }
    float mn = m;
#pragma unroll
    for (int u = 0; u < U; ++u) mn = fmaxf(mn, sc[u]);
    const float alpha = __builtin_amdgcn_exp2f(m - mn);
    m = mn;
    float p[U];
    l *= alpha;
#pragma unroll
    for (int u = 0; u < U; ++u) { p[u] = __builtin_amdgcn_exp2f(sc[u] - mn); l += p[u]; }
#pragma unroll
    for (int k = 0; k < 24; ++k) acc[k] *= alpha;
#pragma unroll
    for (int u = 0; u < U; ++u)
#pragma unroll
      for (int c = 0; c < 3; ++c)
#pragma unroll
        for (int k = 0; k < 4; ++k) {
          acc[c * 8 + 2 * k] = fmaf(p[u], lo_f(Vc[u][c][k]), acc[c * 8 + 2 * k]);
          acc[c * 8 + 2 * k + 1] = fmaf(p[u], hi_f(Vc[u][c][k]), acc[c * 8 + 2 * k + 1]);
        }
  }
#undef ISSUE
  float M = m;
  M = fmaxf(M, __shfl_xor(M, 8)); M = fmaxf(M, __shfl_xor(M, 16)); M = fmaxf(M, __shfl_xor(M, 32));
  const float w = __builtin_amdgcn_exp2f(m - M);
  l *= w;
  l += __shfl_xor(l, 8); l += __shfl_xor(l, 16); l += __shfl_xor(l, 32);
#pragma unroll
  for (int k = 0; k < 24; ++k) { float a = acc[k] * w; a += __shfl_xor(a, 8); a += __shfl_xor(a, 16); a += __shfl_xor(a, 32); acc[k] = a; }
  if (g == 0) {
#pragma unroll
    for (int c = 0; c < 3; ++c)
#pragma unroll
      for (int k = 0; k < 8; k += 4)
        *reinterpret_cast<f32x4*>(&mrg[wave][4 + (i + 8 * c) * 8 + k]) = f32x4{acc[c * 8 + k], acc[c * 8 + k + 1], acc[c * 8 + k + 2], acc[c * 8 + k + 3]};
    if (i == 0) { mrg[wave][0] = M; mrg[wave][1] = l; }
  }
  __syncthreads();
  if (tid < DH) {
    float MM = -INFINITY;
#pragma unroll
    for (int ww = 0; ww < NW; ++ww) MM = fmaxf(MM, mrg[ww][0]);
    float L = 0.f, o = 0.f;
#pragma unroll
    for (int ww = 0; ww < NW; ++ww) { const float wt = __builtin_amdgcn_exp2f(mrg[ww][0] - MM); L = fmaf(wt, mrg[ww][1], L); o = fmaf(wt, mrg[ww][4 + tid], o); }
    po[((size_t)h * NSPLIT + split) * DH + tid] = o;
    if (tid == 0) { pml[(h * NSPLIT + split) * 2] = MM; pml[(h * NSPLIT + split) * 2 + 1] = L; }
  }
}

__global__ void __launch_bounds__(256) k3_out(const float* __restrict__ pml, const float* __restrict__ po, const float* __restrict__ hbuf,
                                              const uint16_t* __restrict__ wo, const uint16_t* __restrict__ w2, float* __restrict__ sp) {
  __shared__ float ov[DH];
  __shared__ float part[4][64];
  const int cg = blockIdx.x, rg = blockIdx.y;
  const int tid = threadIdx.x, lane = tid & 63, wave = tid >> 6, g = lane >> 3, i = lane & 7;
  const bool isw = rg < NH;
  const uint16_t* wp = isw ? wo + (size_t)rg * DH * DM : w2 + (size_t)(rg - NH) * DH * DM;
  u32x4 wr[6];
#pragma unroll
  for (int it = 0; it < 6; ++it) {
    const int r = wave * 48 + it * 8 + g;
    wr[it] = __builtin_nontemporal_load(reinterpret_cast<const u32x4*>(wp + (size_t)r * DM + cg * 64 + i * 8));
  }
  if (tid < DH) {
    if (isw) {
      float MM = -INFINITY;
      for (int s = 0; s < NSPLIT; ++s) MM = fmaxf(MM, pml[(rg * NSPLIT + s) * 2]);
      float L = 0.f, o = 0.f;
      for (int s = 0; s < NSPLIT; ++s) {
        const float wt = __builtin_amdgcn_exp2f(pml[(rg * NSPLIT + s) * 2] - MM);
        L = fmaf(wt, pml[(rg * NSPLIT + s) * 2 + 1], L);
        o = fmaf(wt, po[((size_t)rg * NSPLIT + s) * DH + tid], o);
      }
      ov[tid] = rbf(o / L);
    } else {
      ov[tid] = hbuf[(rg - NH) * DH + tid];
    }
  }
  __syncthreads();
  float acc[8];
#pragma unroll
  for (int k = 0; k < 8; ++k) acc[k] = 0.f;
#pragma unroll
  for (int it = 0; it < 6; ++it) {
    const float xv = ov[wave * 48 + it * 8 + g];
#pragma unroll
    for (int k = 0; k < 4; ++k) { acc[2 * k] = fmaf(xv, lo_f(wr[it][k]), acc[2 * k]); acc[2 * k + 1] = fmaf(xv, hi_f(wr[it][k]), acc[2 * k + 1]); }
  }
#pragma unroll
  for (int k = 0; k < 8; ++k) { acc[k] += __shfl_xor(acc[k], 8); acc[k] += __shfl_xor(acc[k], 16); acc[k] += __shfl_xor(acc[k], 32); }
  if (g == 0) {
#pragma unroll
    for (int k = 0; k < 8; ++k) part[wave][i * 8 + k] = acc[k];
  }
  __syncthreads();
  if (tid < 64) sp[(size_t)rg * DM + cg * 64 + tid] = part[0][tid] + part[1][tid] + part[2][tid] + part[3][tid];
}

__global__ void __launch_bounds__(640) k4_fin(const uint16_t* __restrict__ x, const float* __restrict__ sp, uint16_t* __restrict__ out) {
  const int c = threadIdx.x;
  float s = 0.f, mo = 0.f;
#pragma unroll
  for (int r = 0; r < NH; ++r) s += sp[(size_t)r * DM + c];
#pragma unroll
  for (int r = NH; r < NH + 8; ++r) mo += sp[(size_t)r * DM + c];
  const float sb = rbf(s), mb = rbf(mo);
  const float gg = rbf(mb / (1.f + __expf(-mb)));
  out[c] = f2bf(rbf(bf2f(x[c]) + rbf(sb * gg)));
}

extern "C" void kernel_launch(void* const* d_in, const int* in_sizes, int n_in,
                              void* d_out, int out_size, void* d_ws, size_t ws_size,
                              hipStream_t stream) {
  const uint16_t* x = (const uint16_t*)d_in[0];
  const uint16_t* kc = (const uint16_t*)d_in[1];
  const uint16_t* vc = (const uint16_t*)d_in[2];
  const uint16_t* wq = (const uint16_t*)d_in[3];
  const uint16_t* wo = (const uint16_t*)d_in[4];
  const uint16_t* w1 = (const uint16_t*)d_in[6];
  const uint16_t* w3 = (const uint16_t*)d_in[7];
  const uint16_t* w2 = (const uint16_t*)d_in[8];
  char* ws = (char*)d_ws;
  uint16_t* q = (uint16_t*)(ws + WS_Q);
  float* hb = (float*)(ws + WS_H);
  float* pml = (float*)(ws + WS_PML);
  float* po = (float*)(ws + WS_PO);
  float* sp = (float*)(ws + WS_SP);
  k1_proj<<<dim3(96 + 24), dim3(512), 0, stream>>>(x, wq, w1, w3, q, hb);
  k2_attn<<<dim3(NSPLIT, NH), dim3(NW * 64), 0, stream>>>(q, kc, vc, pml, po);
  k3_out<<<dim3(10, NH + 8), dim3(256), 0, stream>>>(pml, po, hb, wo, w2, sp);
  k4_fin<<<dim3(1), dim3(640), 0, stream>>>(x, sp, (uint16_t*)d_out);
}
```

```cpp
#include <hip/hip_runtime.h>
#include <hip/hip_bf16.h>
#include <cstdint>

constexpr int DM = 640, NH = 32, SK = 32768, DH = 192, FF = 1536, HD = NH * DH;

#define K2_NSPLIT 16
#define K2_NW 8
#define K2_U 2
constexpr int NSPLIT = K2_NSPLIT, NW = K2_NW, U = K2_U;
constexpr int KPW = SK / NSPLIT / NW;
static_assert(KPW % (8 * U) == 0, "");

constexpr int RS = 4;
constexpr size_t WS_QP = 0;
constexpr size_t WS_GP = WS_QP + (size_t)RS * HD * 4;
constexpr size_t WS_UP = WS_GP + (size_t)RS * FF * 4;
constexpr size_t WS_CNT = WS_UP + (size_t)RS * FF * 4;
constexpr size_t WS_PML = WS_CNT + 256;
constexpr size_t WS_PO = WS_PML + (size_t)NH * NSPLIT * 2 * 4;
constexpr size_t WS_SP = WS_PO + (size_t)NH * NSPLIT * DH * 4;

using u32x4 = __attribute__((ext_vector_type(4))) unsigned;
using f32x4 = __attribute__((ext_vector_type(4))) float;
using bf16x2 = __attribute__((ext_vector_type(2))) __bf16;

__device__ __forceinline__ float bf2f(uint16_t b) { return __builtin_bit_cast(float, (unsigned)b << 16); }
__device__ __forceinline__ uint16_t f2bf(float f) {
  unsigned u = __builtin_bit_cast(unsigned, f);
  u += 0x7FFFu + ((u >> 16) & 1u);
  return (uint16_t)(u >> 16);
}
__device__ __forceinline__ float rbf(float f) { return bf2f(f2bf(f)); }
__device__ __forceinline__ float dot2(unsigned a, unsigned b, float acc) {
  return __builtin_amdgcn_fdot2_f32_bf16(__builtin_bit_cast(bf16x2, a), __builtin_bit_cast(bf16x2, b), acc, false);
}
template <int CTRL> __device__ __forceinline__ float dpp(float x) {
  return __builtin_bit_cast(float, __builtin_amdgcn_mov_dpp(__builtin_bit_cast(int, x), CTRL, 0xf, 0xf, true));
}
__device__ __forceinline__ float lo_f(unsigned w) { return __builtin_bit_cast(float, w << 16); }
__device__ __forceinline__ float hi_f(unsigned w) { return __builtin_bit_cast(float, w & 0xffff0000u); }

__global__ void __launch_bounds__(256) k1_proj(const uint16_t* __restrict__ x, const uint16_t* __restrict__ wq,
                                               const uint16_t* __restrict__ w1, const uint16_t* __restrict__ w3,
                                               float* __restrict__ qp, float* __restrict__ gp, unsigned* __restrict__ cnt) {
  __shared__ float xn_s[DM];
  __shared__ float red[8];
  __shared__ float part[4][64];
  const int tid = threadIdx.x, lane = tid & 63, wave = tid >> 6, g = lane >> 3, i = lane & 7;
  const int ct = blockIdx.x, rg = blockIdx.y;
  const uint16_t* wsrc; int ncol, col0; float* dst;
  if (ct < 96) { wsrc = wq; ncol = HD; col0 = ct * 64; dst = qp + (size_t)rg * HD + col0; }
  else if (ct < 120) { wsrc = w1; ncol = FF; col0 = (ct - 96) * 64; dst = gp + (size_t)rg * FF + col0; }
  else { wsrc = w3; ncol = FF; col0 = (ct - 120) * 64; dst = gp + (size_t)RS * FF + (size_t)rg * FF + col0; }
  constexpr int RPW = DM / RS / 4;
  const int rbase = rg * (DM / RS) + wave * RPW;
  u32x4 wr[RPW / 8];
#pragma unroll
  for (int it = 0; it < RPW / 8; ++it)
    wr[it] = __builtin_nontemporal_load(reinterpret_cast<const u32x4*>(wsrc + (size_t)(rbase + it * 8 + g) * ncol + col0 + i * 8));
  if (ct == 0 && rg == 0 && tid < 16) cnt[tid] = 0u;
  float xv[3];
#pragma unroll
  for (int k = 0; k < 3; ++k) xv[k] = (tid + 256 * k < DM) ? bf2f(x[tid + 256 * k]) : 0.f;
  float s = xv[0] + xv[1] + xv[2];
#pragma unroll
  for (int o = 32; o >= 1; o >>= 1) s += __shfl_xor(s, o);
  if (lane == 0) red[wave] = s;
  __syncthreads();
  const float mean = (red[0] + red[1] + red[2] + red[3]) * (1.f / DM);
  float s2 = 0.f;
#pragma unroll
  for (int k = 0; k < 3; ++k) { const float d = (tid + 256 * k < DM) ? xv[k] - mean : 0.f; s2 = fmaf(d, d, s2); }
#pragma unroll
  for (int o = 32; o >= 1; o >>= 1) s2 += __shfl_xor(s2, o);
  if (lane == 0) red[4 + wave] = s2;
  __syncthreads();
  const float mb = rbf(mean), vb = rbf((red[4] + red[5] + red[6] + red[7]) * (1.f / DM));
  const float den = rbf(sqrtf(rbf(vb + 1e-5f)));
#pragma unroll
  for (int k = 0; k < 3; ++k) if (tid + 256 * k < DM) xn_s[tid + 256 * k] = rbf(rbf(xv[k] - mb) / den);
  __syncthreads();
  float acc[8];
#pragma unroll
  for (int k = 0; k < 8; ++k) acc[k] = 0.f;
#pragma unroll
  for (int it = 0; it < RPW / 8; ++it) {
    const float xs = xn_s[rbase + it * 8 + g];
#pragma unroll
    for (int k = 0; k < 4; ++k) { acc[2 * k] = fmaf(xs, lo_f(wr[it][k]), acc[2 * k]); acc[2 * k + 1] = fmaf(xs, hi_f(wr[it][k]), acc[2 * k + 1]); }
  }
#pragma unroll
  for (int k = 0; k < 8; ++k) { acc[k] += __shfl_xor(acc[k], 8); acc[k] += __shfl_xor(acc[k], 16); acc[k] += __shfl_xor(acc[k], 32); }
  if (g == 0) {
#pragma unroll
    for (int k = 0; k < 8; ++k) part[wave][i * 8 + k] = acc[k];
  }
  __syncthreads();
  if (tid < 64) dst[tid] = part[0][tid] + part[1][tid] + part[2][tid] + part[3][tid];
}

__global__ void __launch_bounds__(NW * 64) k2_attn(const float* __restrict__ qp, const uint16_t* __restrict__ kc,
                                                   const uint16_t* __restrict__ vc, float* __restrict__ pml, float* __restrict__ po) {
  __shared__ __attribute__((aligned(16))) float mrg[NW][DH + 4];
  const int split = blockIdx.x, h = blockIdx.y;
  const int tid = threadIdx.x, lane = tid & 63, wave = tid >> 6, g = lane >> 3, i = lane & 7;
  constexpr float C2 = 0.07216878364870322f * 1.4426950408889634f;
  const size_t row0 = (size_t)h * SK + (size_t)(split * NW + wave) * KPW + g;
  const uint16_t* kp = kc + row0 * DH + i * 8;
  const uint16_t* vp = vc + row0 * DH + i * 8;
  u32x4 qv[3];
#pragma unroll
  for (int c = 0; c < 3; ++c) {
    f32x4 a0 = {0.f, 0.f, 0.f, 0.f}, a1 = {0.f, 0.f, 0.f, 0.f};
#pragma unroll
    for (int r = 0; r < RS; ++r) {
      const float* qq = qp + (size_t)r * HD + h * DH + (i + 8 * c) * 8;
      a0 += *reinterpret_cast<const f32x4*>(qq); a1 += *reinterpret_cast<const f32x4*>(qq + 4);
    }
    qv[c] = u32x4{(unsigned)f2bf(a0[0]) | ((unsigned)f2bf(a0[1]) << 16), (unsigned)f2bf(a0[2]) | ((unsigned)f2bf(a0[3]) << 16),
                  (unsigned)f2bf(a1[0]) | ((unsigned)f2bf(a1[1]) << 16), (unsigned)f2bf(a1[2]) | ((unsigned)f2bf(a1[3]) << 16)};
  }
  u32x4 Kn[U][3], Vn[U][3];
#define ISSUE(t_) do { _Pragma("unroll") for (int u_ = 0; u_ < U; ++u_) _Pragma("unroll") for (int c_ = 0; c_ < 3; ++c_) { \
      Kn[u_][c_] = __builtin_nontemporal_load(reinterpret_cast<const u32x4*>(kp + ((size_t)(t_) * 8 * U + u_ * 8) * DH + c_ * 64)); } \
    _Pragma("unroll") for (int u_ = 0; u_ < U; ++u_) _Pragma("unroll") for (int c_ = 0; c_ < 3; ++c_) { \
      Vn[u_][c_] = __builtin_nontemporal_load(reinterpret_cast<const u32x4*>(vp + ((size_t)(t_) * 8 * U + u_ * 8) * DH + c_ * 64)); } } while (0)
  float m = -INFINITY, l = 0.f, acc[24];
#pragma unroll
  for (int k = 0; k < 24; ++k) acc[k] = 0.f;
  constexpr int NT = KPW / (8 * U);
  ISSUE(0);
  for (int t = 0; t < NT; ++t) {
    u32x4 Kc[U][3], Vc[U][3];
#pragma unroll
    for (int u = 0; u < U; ++u)
#pragma unroll
      for (int c = 0; c < 3; ++c) { Kc[u][c] = Kn[u][c]; Vc[u][c] = Vn[u][c]; }
    if (t + 1 < NT) ISSUE(t + 1);
    float sc[U];
#pragma unroll
    for (int u = 0; u < U; ++u) {
      float d = 0.f;
#pragma unroll
      for (int c = 0; c < 3; ++c)
#pragma unroll
        for (int k = 0; k < 4; ++k) d = dot2(Kc[u][c][k], qv[c][k], d);
      d += dpp<0xB1>(d); d += dpp<0x4E>(d); d += dpp<0x141>(d);
      sc[u] = d * C2;
    }
    float mn = m;
#pragma unroll
    for (int u = 0; u < U; ++u) mn = fmaxf(mn, sc[u]);
    const float alpha = __builtin_amdgcn_exp2f(m - mn);
    m = mn;
    float p[U];
    l *= alpha;
#pragma unroll
    for (int u = 0; u < U; ++u) { p[u] = __builtin_amdgcn_exp2f(sc[u] - mn); l += p[u]; }
#pragma unroll
    for (int k = 0; k < 24; ++k) acc[k] *= alpha;
#pragma unroll
    for (int u = 0; u < U; ++u)
#pragma unroll
      for (int c = 0; c < 3; ++c)
#pragma unroll
        for (int k = 0; k < 4; ++k) {
          acc[c * 8 + 2 * k] = fmaf(p[u], lo_f(Vc[u][c][k]), acc[c * 8 + 2 * k]);
          acc[c * 8 + 2 * k + 1] = fmaf(p[u], hi_f(Vc[u][c][k]), acc[c * 8 + 2 * k + 1]);
        }
  }
#undef ISSUE
  float M = m;
  M = fmaxf(M, __shfl_xor(M, 8)); M = fmaxf(M, __shfl_xor(M, 16)); M = fmaxf(M, __shfl_xor(M, 32));
  const float w = __builtin_amdgcn_exp2f(m - M);
  l *= w;
  l += __shfl_xor(l, 8); l += __shfl_xor(l, 16); l += __shfl_xor(l, 32);
#pragma unroll
  for (int k = 0; k < 24; ++k) { float a = acc[k] * w; a += __shfl_xor(a, 8); a += __shfl_xor(a, 16); a += __shfl_xor(a, 32); acc[k] = a; }
  if (g == 0) {
#pragma unroll
    for (int c = 0; c < 3; ++c)
#pragma unroll
      for (int k = 0; k < 8; k += 4)
        *reinterpret_cast<f32x4*>(&mrg[wave][4 + (i + 8 * c) * 8 + k]) = f32x4{acc[c * 8 + k], acc[c * 8 + k + 1], acc[c * 8 + k + 2], acc[c * 8 + k + 3]};
    if (i == 0) { mrg[wave][0] = M; mrg[wave][1] = l; }
  }
  __syncthreads();
  if (tid < DH) {
    float MM = -INFINITY;
#pragma unroll
    for (int ww = 0; ww < NW; ++ww) MM = fmaxf(MM, mrg[ww][0]);
    float L = 0.f, o = 0.f;
#pragma unroll
    for (int ww = 0; ww < NW; ++ww) { const float wt = __builtin_amdgcn_exp2f(mrg[ww][0] - MM); L = fmaf(wt, mrg[ww][1], L); o = fmaf(wt, mrg[ww][4 + tid], o); }
    po[((size_t)h * NSPLIT + split) * DH + tid] = o;
    if (tid == 0) { pml[(h * NSPLIT + split) * 2] = MM; pml[(h * NSPLIT + split) * 2 + 1] = L; }
  }
}

constexpr int NRG = NH + FF / DH;
__global__ void __launch_bounds__(256) k3_out(const float* __restrict__ pml, const float* __restrict__ po, const float* __restrict__ gp,
                                              const uint16_t* __restrict__ wo, const uint16_t* __restrict__ w2, float* sp, unsigned* cnt,
                                              const uint16_t* __restrict__ x, uint16_t* __restrict__ out) {
  __shared__ float ov[DH];
  __shared__ float part[4][64];
  __shared__ unsigned lastf;
  const int cg = blockIdx.x, rg = blockIdx.y;
  const int tid = threadIdx.x, lane = tid & 63, wave = tid >> 6, g = lane >> 3, i = lane & 7;
  const bool isw = rg < NH;
  const uint16_t* wp = isw ? wo + (size_t)rg * DH * DM : w2 + (size_t)(rg - NH) * DH * DM;
  u32x4 wr[6];
#pragma unroll
  for (int it = 0; it < 6; ++it) {
    const int r = wave * 48 + it * 8 + g;
    wr[it] = __builtin_nontemporal_load(reinterpret_cast<const u32x4*>(wp + (size_t)r * DM + cg * 64 + i * 8));
  }
  if (tid < DH) {
    if (isw) {
      float mv[NSPLIT], lv[NSPLIT], ovv[NSPLIT];
#pragma unroll
      for (int s = 0; s < NSPLIT; ++s) { mv[s] = pml[(rg * NSPLIT + s) * 2]; lv[s] = pml[(rg * NSPLIT + s) * 2 + 1]; ovv[s] = po[((size_t)rg * NSPLIT + s) * DH + tid]; }
      float MM = -INFINITY;
#pragma unroll
      for (int s = 0; s < NSPLIT; ++s) MM = fmaxf(MM, mv[s]);
      float L = 0.f, o = 0.f;
#pragma unroll
      for (int s = 0; s < NSPLIT; ++s) { const float wt = __builtin_amdgcn_exp2f(mv[s] - MM); L = fmaf(wt, lv[s], L); o = fmaf(wt, ovv[s], o); }
      ov[tid] = rbf(o / L);
    } else {
      const int f = (rg - NH) * DH + tid;
      float gt = 0.f, up = 0.f;
#pragma unroll
      for (int r = 0; r < RS; ++r) { gt += gp[(size_t)r * FF + f]; up += gp[(size_t)(RS + r) * FF + f]; }
      gt = rbf(gt); up = rbf(up);
      const float sl = rbf(gt / (1.f + __expf(-gt)));
      ov[tid] = rbf(sl * up);
    }
  }
  __syncthreads();
  float acc[8];
#pragma unroll
  for (int k = 0; k < 8; ++k) acc[k] = 0.f;
#pragma unroll
  for (int it = 0; it < 6; ++it) {
    const float xv = ov[wave * 48 + it * 8 + g];
#pragma unroll
    for (int k = 0; k < 4; ++k) { acc[2 * k] = fmaf(xv, lo_f(wr[it][k]), acc[2 * k]); acc[2 * k + 1] = fmaf(xv, hi_f(wr[it][k]), acc[2 * k + 1]); }
  }
#pragma unroll
  for (int k = 0; k < 8; ++k) { acc[k] += __shfl_xor(acc[k], 8); acc[k] += __shfl_xor(acc[k], 16); acc[k] += __shfl_xor(acc[k], 32); }
  if (g == 0) {
#pragma unroll
    for (int k = 0; k < 8; ++k) part[wave][i * 8 + k] = acc[k];
  }
  __syncthreads();
  if (tid < 64) {
    const float v = part[0][tid] + part[1][tid] + part[2][tid] + part[3][tid];
    __hip_atomic_store(reinterpret_cast<unsigned*>(sp) + (size_t)rg * DM + cg * 64 + tid, __builtin_bit_cast(unsigned, v), __ATOMIC_RELAXED, __HIP_MEMORY_SCOPE_AGENT);
    asm volatile("s_waitcnt vmcnt(0)" ::: "memory");
    if (tid == 0) {
      const unsigned old = __hip_atomic_fetch_add(cnt + cg, 1u, __ATOMIC_RELAXED, __HIP_MEMORY_SCOPE_AGENT);
      lastf = (old == (unsigned)(NRG - 1)) ? 1u : 0u;
    }
  }
  __syncthreads();
  if (lastf == 0u || tid >= 64) return;
  const int c = cg * 64 + tid;
  float vals[NRG];
#pragma unroll
  for (int r = 0; r < NRG; ++r) vals[r] = __builtin_bit_cast(float, __hip_atomic_load(reinterpret_cast<unsigned*>(sp) + (size_t)r * DM + c, __ATOMIC_RELAXED, __HIP_MEMORY_SCOPE_AGENT));
  float s = 0.f, mo = 0.f;
#pragma unroll
  for (int r = 0; r < NH; ++r) s += vals[r];
#pragma unroll
  for (int r = NH; r < NRG; ++r) mo += vals[r];
  const float sb = rbf(s), mb = rbf(mo);
  const float gg = rbf(mb / (1.f + __expf(-mb)));
  out[c] = f2bf(rbf(bf2f(x[c]) + rbf(sb * gg)));
}

extern "C" void kernel_launch(void* const* d_in, const int* in_sizes, int n_in,
                              void* d_out, int out_size, void* d_ws, size_t ws_size,
                              hipStream_t stream) {
  const uint16_t* x = (const uint16_t*)d_in[0];
  const uint16_t* kc = (const uint16_t*)d_in[1];
  const uint16_t* vc = (const uint16_t*)d_in[2];
  const uint16_t* wq = (const uint16_t*)d_in[3];
  const uint16_t* wo = (const uint16_t*)d_in[4];
  const uint16_t* w1 = (const uint16_t*)d_in[6];
  const uint16_t* w3 = (const uint16_t*)d_in[7];
  const uint16_t* w2 = (const uint16_t*)d_in[8];
  char* ws = (char*)d_ws;
  float* qp = (float*)(ws + WS_QP);
  float* gp = (float*)(ws + WS_GP);
  unsigned* cnt = (unsigned*)(ws + WS_CNT);
  float* pml = (float*)(ws + WS_PML);
  float* po = (float*)(ws + WS_PO);
  float* sp = (float*)(ws + WS_SP);
  k1_proj<<<dim3(96 + 48, RS), dim3(256), 0, stream>>>(x, wq, w1, w3, qp, gp, cnt);
  k2_attn<<<dim3(NSPLIT, NH), dim3(NW * 64), 0, stream>>>(qp, kc, vc, pml, po);
  k3_out<<<dim3(10, NRG), dim3(256), 0, stream>>>(pml, po, gp, wo, w2, sp, cnt, x, (uint16_t*)d_out);
}
```

```cpp
#include <hip/hip_runtime.h>
#include <hip/hip_bf16.h>
#include <cstdint>

constexpr int DM = 640, NH = 32, SK = 32768, DH = 192, FF = 1536, HD = NH * DH;

constexpr int NSPLIT = 8, NW = 8;
#define K2_U 2
constexpr int U = K2_U;
constexpr int KPW = SK / NSPLIT / NW;
static_assert(KPW % (8 * U) == 0, "");
constexpr int NATT = NH * NSPLIT;
constexpr int NGLU = (FF / DH) * 8;
constexpr int CGW = DM / 8;

constexpr int RS = 4;
constexpr size_t WS_QP = 0;
constexpr size_t WS_GP = WS_QP + (size_t)RS * HD * 4;
constexpr size_t WS_PG = WS_GP + (size_t)2 * RS * FF * 4;
constexpr size_t WS_SL = WS_PG + (size_t)NH * NSPLIT * 196 * 8;
constexpr size_t WS_GEND = WS_SL + (size_t)40 * DM * 8;
constexpr int ZERO16 = (int)((WS_GEND - WS_PG) / 16);
constexpr unsigned TAG = 0x5A5A0001u;
typedef __attribute__((address_space(1))) unsigned long long gu64;

using u32x4 = __attribute__((ext_vector_type(4))) unsigned;
using f32x4 = __attribute__((ext_vector_type(4))) float;
using bf16x2 = __attribute__((ext_vector_type(2))) __bf16;

__device__ __forceinline__ float bf2f(uint16_t b) { return __builtin_bit_cast(float, (unsigned)b << 16); }
__device__ __forceinline__ uint16_t f2bf(float f) {
  unsigned u = __builtin_bit_cast(unsigned, f);
  u += 0x7FFFu + ((u >> 16) & 1u);
  return (uint16_t)(u >> 16);
}
__device__ __forceinline__ float rbf(float f) { return bf2f(f2bf(f)); }
__device__ __forceinline__ float dot2(unsigned a, unsigned b, float acc) {
  return __builtin_amdgcn_fdot2_f32_bf16(__builtin_bit_cast(bf16x2, a), __builtin_bit_cast(bf16x2, b), acc, false);
}
template <int CTRL> __device__ __forceinline__ float dpp(float x) {
  return __builtin_bit_cast(float, __builtin_amdgcn_mov_dpp(__builtin_bit_cast(int, x), CTRL, 0xf, 0xf, true));
}
__device__ __forceinline__ float lo_f(unsigned w) { return __builtin_bit_cast(float, w << 16); }
__device__ __forceinline__ float hi_f(unsigned w) { return __builtin_bit_cast(float, w & 0xffff0000u); }

__global__ void __launch_bounds__(256) k1_proj(const uint16_t* __restrict__ x, const uint16_t* __restrict__ wq,
                                               const uint16_t* __restrict__ w1, const uint16_t* __restrict__ w3,
                                               float* __restrict__ qp, float* __restrict__ gp, u32x4* __restrict__ zmem) {
  __shared__ float xn_s[DM];
  __shared__ float red[8];
  __shared__ float part[4][64];
  const int tid = threadIdx.x, lane = tid & 63, wave = tid >> 6, g = lane >> 3, i = lane & 7;
  const int ct = blockIdx.x, rg = blockIdx.y;
  const uint16_t* wsrc; int ncol, col0; float* dst;
  if (ct < 96) { wsrc = wq; ncol = HD; col0 = ct * 64; dst = qp + (size_t)rg * HD + col0; }
  else if (ct < 120) { wsrc = w1; ncol = FF; col0 = (ct - 96) * 64; dst = gp + (size_t)rg * FF + col0; }
  else { wsrc = w3; ncol = FF; col0 = (ct - 120) * 64; dst = gp + (size_t)RS * FF + (size_t)rg * FF + col0; }
  constexpr int RPW = DM / RS / 4;
  const int rbase = rg * (DM / RS) + wave * RPW;
  u32x4 wr[RPW / 8];
#pragma unroll
  for (int it = 0; it < RPW / 8; ++it)
    wr[it] = __builtin_nontemporal_load(reinterpret_cast<const u32x4*>(wsrc + (size_t)(rbase + it * 8 + g) * ncol + col0 + i * 8));
  { const int gid = (rg * 144 + ct) * 256 + tid; if (gid < ZERO16) zmem[gid] = u32x4{0u, 0u, 0u, 0u}; }
  float xv[3];
#pragma unroll
  for (int k = 0; k < 3; ++k) xv[k] = (tid + 256 * k < DM) ? bf2f(x[tid + 256 * k]) : 0.f;
  float s = xv[0] + xv[1] + xv[2];
#pragma unroll
  for (int o = 32; o >= 1; o >>= 1) s += __shfl_xor(s, o);
  if (lane == 0) red[wave] = s;
  __syncthreads();
  const float mean = (red[0] + red[1] + red[2] + red[3]) * (1.f / DM);
  float s2 = 0.f;
#pragma unroll
  for (int k = 0; k < 3; ++k) { const float d = (tid + 256 * k < DM) ? xv[k] - mean : 0.f; s2 = fmaf(d, d, s2); }
#pragma unroll
  for (int o = 32; o >= 1; o >>= 1) s2 += __shfl_xor(s2, o);
  if (lane == 0) red[4 + wave] = s2;
  __syncthreads();
  const float mb = rbf(mean), vb = rbf((red[4] + red[5] + red[6] + red[7]) * (1.f / DM));
  const float den = rbf(sqrtf(rbf(vb + 1e-5f)));
#pragma unroll
  for (int k = 0; k < 3; ++k) if (tid + 256 * k < DM) xn_s[tid + 256 * k] = rbf(rbf(xv[k] - mb) / den);
  __syncthreads();
  float acc[8];
#pragma unroll
  for (int k = 0; k < 8; ++k) acc[k] = 0.f;
#pragma unroll
  for (int it = 0; it < RPW / 8; ++it) {
    const float xs = xn_s[rbase + it * 8 + g];
#pragma unroll
    for (int k = 0; k < 4; ++k) { acc[2 * k] = fmaf(xs, lo_f(wr[it][k]), acc[2 * k]); acc[2 * k + 1] = fmaf(xs, hi_f(wr[it][k]), acc[2 * k + 1]); }
  }
#pragma unroll
  for (int k = 0; k < 8; ++k) { acc[k] += __shfl_xor(acc[k], 8); acc[k] += __shfl_xor(acc[k], 16); acc[k] += __shfl_xor(acc[k], 32); }
  if (g == 0) {
#pragma unroll
    for (int k = 0; k < 8; ++k) part[wave][i * 8 + k] = acc[k];
  }
  __syncthreads();
  if (tid < 64) dst[tid] = part[0][tid] + part[1][tid] + part[2][tid] + part[3][tid];
}

__device__ __forceinline__ void put_granule(unsigned long long* g, float v) {
  __hip_atomic_store((gu64*)g, ((unsigned long long)TAG << 32) | (unsigned long long)__builtin_bit_cast(unsigned, v), __ATOMIC_RELAXED, __HIP_MEMORY_SCOPE_AGENT);
}
__device__ __forceinline__ float get_granule(const unsigned long long* g) {
  unsigned long long x = __hip_atomic_load((gu64*)g, __ATOMIC_RELAXED, __HIP_MEMORY_SCOPE_AGENT);
  for (unsigned spins = 0; (unsigned)(x >> 32) != TAG && spins < (1u << 22); ++spins) {
    __builtin_amdgcn_s_sleep(1);
    x = __hip_atomic_load((gu64*)g, __ATOMIC_RELAXED, __HIP_MEMORY_SCOPE_AGENT);
  }
  return __builtin_bit_cast(float, (unsigned)x);
}

__global__ void __launch_bounds__(NW * 64, 4) k2_attn(const float* __restrict__ qp, const float* __restrict__ gp,
                                                   const uint16_t* __restrict__ kc, const uint16_t* __restrict__ vc,
                                                   const uint16_t* __restrict__ wo, const uint16_t* __restrict__ w2,
                                                   unsigned long long* pg, unsigned long long* sl,
                                                   const uint16_t* __restrict__ x, uint16_t* __restrict__ out) {
  __shared__ __attribute__((aligned(16))) u32x4 wot[DH * 10];
  __shared__ __attribute__((aligned(16))) float mrg[NW][DH + 4];
  __shared__ float red[48][CGW];
  __shared__ float ov[DH];
  const int tid = threadIdx.x, lane = tid & 63, wave = tid >> 6, g = lane >> 3, i = lane & 7;
  const bool att = blockIdx.x < NATT;
  const int j = att ? (blockIdx.x >> 5) : (blockIdx.x & 7);
  const int h = att ? (blockIdx.x & 31) : 0;
  const int rgp = att ? 0 : ((blockIdx.x - NATT) >> 3);
  const int srow = att ? h : NH + rgp;
  {
    const uint16_t* wsrc = (att ? wo + (size_t)h * DH * DM : w2 + (size_t)rgp * DH * DM) + j * CGW;
    if (tid < 480) {
      const int c = tid % 10, rg = tid / 10;
#pragma unroll
      for (int k = 0; k < 4; ++k) { const int r = rg + 48 * k; wot[r * 10 + c] = __builtin_nontemporal_load(reinterpret_cast<const u32x4*>(wsrc + (size_t)r * DM + c * 8)); }
    }
  }
  if (att) {
    constexpr float C2 = 0.07216878364870322f * 1.4426950408889634f;
    const size_t row0 = (size_t)h * SK + (size_t)(j * NW + wave) * KPW + g;
    const uint16_t* kp = kc + row0 * DH + i * 8;
    const uint16_t* vp = vc + row0 * DH + i * 8;
    u32x4 qv[3];
#pragma unroll
    for (int c = 0; c < 3; ++c) {
      f32x4 a0 = {0.f, 0.f, 0.f, 0.f}, a1 = {0.f, 0.f, 0.f, 0.f};
#pragma unroll
      for (int r = 0; r < RS; ++r) {
        const float* qq = qp + (size_t)r * HD + h * DH + (i + 8 * c) * 8;
        a0 += *reinterpret_cast<const f32x4*>(qq); a1 += *reinterpret_cast<const f32x4*>(qq + 4);
      }
      qv[c] = u32x4{(unsigned)f2bf(a0[0]) | ((unsigned)f2bf(a0[1]) << 16), (unsigned)f2bf(a0[2]) | ((unsigned)f2bf(a0[3]) << 16),
                    (unsigned)f2bf(a1[0]) | ((unsigned)f2bf(a1[1]) << 16), (unsigned)f2bf(a1[2]) | ((unsigned)f2bf(a1[3]) << 16)};
    }
    u32x4 Kn[U][3], Vn[U][3];
#define ISSUE(t_) do { _Pragma("unroll") for (int u_ = 0; u_ < U; ++u_) _Pragma("unroll") for (int c_ = 0; c_ < 3; ++c_) { \
      Kn[u_][c_] = __builtin_nontemporal_load(reinterpret_cast<const u32x4*>(kp + ((size_t)(t_) * 8 * U + u_ * 8) * DH + c_ * 64)); } \
    _Pragma("unroll") for (int u_ = 0; u_ < U; ++u_) _Pragma("unroll") for (int c_ = 0; c_ < 3; ++c_) { \
      Vn[u_][c_] = __builtin_nontemporal_load(reinterpret_cast<const u32x4*>(vp + ((size_t)(t_) * 8 * U + u_ * 8) * DH + c_ * 64)); } } while (0)
    float m = -INFINITY, l = 0.f, acc[24];
#pragma unroll
    for (int k = 0; k < 24; ++k) acc[k] = 0.f;
    constexpr int NT = KPW / (8 * U);
    ISSUE(0);
    for (int t = 0; t < NT; ++t) {
      u32x4 Kc[U][3], Vc[U][3];
#pragma unroll
      for (int u = 0; u < U; ++u)
#pragma unroll
        for (int c = 0; c < 3; ++c) { Kc[u][c] = Kn[u][c]; Vc[u][c] = Vn[u][c]; }
      if (t + 1 < NT) ISSUE(t + 1);
      float sc[U];
#pragma unroll
      for (int u = 0; u < U; ++u) {
        float d = 0.f;
#pragma unroll
        for (int c = 0; c < 3; ++c)
#pragma unroll
          for (int k = 0; k < 4; ++k) d = dot2(Kc[u][c][k], qv[c][k], d);
        d += dpp<0xB1>(d); d += dpp<0x4E>(d); d += dpp<0x141>(d);
        sc[u] = rbf(rbf(d) * 0.07216878364870322f) * 1.4426950408889634f;
      }
      float mn = m;
#pragma unroll
      for (int u = 0; u < U; ++u) mn = fmaxf(mn, sc[u]);
      const float alpha = __builtin_amdgcn_exp2f(m - mn);
      m = mn;
      float p[U];
      l *= alpha;
#pragma unroll
      for (int u = 0; u < U; ++u) { p[u] = __builtin_amdgcn_exp2f(sc[u] - mn); l += p[u]; }
#pragma unroll
      for (int k = 0; k < 24; ++k) acc[k] *= alpha;
#pragma unroll
      for (int u = 0; u < U; ++u)
#pragma unroll
        for (int c = 0; c < 3; ++c)
#pragma unroll
          for (int k = 0; k < 4; ++k) {
            acc[c * 8 + 2 * k] = fmaf(p[u], lo_f(Vc[u][c][k]), acc[c * 8 + 2 * k]);
            acc[c * 8 + 2 * k + 1] = fmaf(p[u], hi_f(Vc[u][c][k]), acc[c * 8 + 2 * k + 1]);
          }
    }
#undef ISSUE
    float M = m;
    M = fmaxf(M, __shfl_xor(M, 8)); M = fmaxf(M, __shfl_xor(M, 16)); M = fmaxf(M, __shfl_xor(M, 32));
    const float w = __builtin_amdgcn_exp2f(m - M);
    l *= w;
    l += __shfl_xor(l, 8); l += __shfl_xor(l, 16); l += __shfl_xor(l, 32);
#pragma unroll
    for (int k = 0; k < 24; ++k) { float a = acc[k] * w; a += __shfl_xor(a, 8); a += __shfl_xor(a, 16); a += __shfl_xor(a, 32); acc[k] = a; }
    if (g == 0) {
#pragma unroll
      for (int c = 0; c < 3; ++c)
#pragma unroll
        for (int k = 0; k < 8; k += 4)
          *reinterpret_cast<f32x4*>(&mrg[wave][4 + (i + 8 * c) * 8 + k]) = f32x4{acc[c * 8 + k], acc[c * 8 + k + 1], acc[c * 8 + k + 2], acc[c * 8 + k + 3]};
      if (i == 0) { mrg[wave][0] = M; mrg[wave][1] = l; }
    }
    __syncthreads();
    unsigned long long* pgh = pg + (size_t)h * NSPLIT * 196;
    if (tid < DH + 2) {
      float MM = -INFINITY;
#pragma unroll
      for (int ww = 0; ww < NW; ++ww) MM = fmaxf(MM, mrg[ww][0]);
      float L = 0.f, o = 0.f;
      const int e = tid < DH ? 4 + tid : 1;
#pragma unroll
      for (int ww = 0; ww < NW; ++ww) { const float wt = __builtin_amdgcn_exp2f(mrg[ww][0] - MM); o = fmaf(wt, mrg[ww][e], o); }
      put_granule(pgh + j * 196 + tid, tid == DH + 1 ? MM : o);
    }
    __syncthreads();
#pragma unroll
    for (int k = 0; k < 4; ++k) {
      const int idx = tid + 512 * k;
      if (idx < NSPLIT * 194) { const int sp = idx / 194, e = idx - sp * 194; mrg[sp][e] = get_granule(pgh + sp * 196 + e); }
    }
    __syncthreads();
    if (tid < DH) {
      float MM = -INFINITY;
#pragma unroll
      for (int sp = 0; sp < NSPLIT; ++sp) MM = fmaxf(MM, mrg[sp][193]);
      float L = 0.f, o = 0.f;
#pragma unroll
      for (int sp = 0; sp < NSPLIT; ++sp) { const float wt = __builtin_amdgcn_exp2f(mrg[sp][193] - MM); L = fmaf(wt, mrg[sp][192], L); o = fmaf(wt, mrg[sp][tid], o); }
      ov[tid] = rbf(o / L);
    }
  } else {
    if (tid < DH) {
      const int f = rgp * DH + tid;
      float gt = 0.f, up = 0.f;
#pragma unroll
      for (int r = 0; r < RS; ++r) { gt += gp[(size_t)r * FF + f]; up += gp[(size_t)(RS + r) * FF + f]; }
      gt = rbf(gt); up = rbf(up);
      const float sl_ = rbf(gt / (1.f + __expf(-gt)));
      ov[tid] = rbf(sl_ * up);
    }
  }
  __syncthreads();
  if (tid < 480) {
    const int c = tid % 10, rg = tid / 10;
    float a8[8];
#pragma unroll
    for (int k = 0; k < 8; ++k) a8[k] = 0.f;
#pragma unroll
    for (int k = 0; k < 4; ++k) {
      const int r = rg + 48 * k; const float xv = ov[r]; const u32x4 wv = wot[r * 10 + c];
#pragma unroll
      for (int e = 0; e < 4; ++e) { a8[2 * e] = fmaf(xv, lo_f(wv[e]), a8[2 * e]); a8[2 * e + 1] = fmaf(xv, hi_f(wv[e]), a8[2 * e + 1]); }
    }
#pragma unroll
    for (int k = 0; k < 8; ++k) red[rg][c * 8 + k] = a8[k];
  }
  __syncthreads();
  if (tid < CGW) {
    float v = 0.f;
#pragma unroll
    for (int r = 0; r < 48; ++r) v += red[r][tid];
    put_granule(sl + (size_t)srow * DM + j * CGW + tid, v);
  }
  if (!(att && h == NH - 1)) return;
  __syncthreads();
  float* fin = &red[0][0];
#pragma unroll
  for (int k = 0; k < 7; ++k) {
    const int idx = tid + 512 * k;
    if (idx < 40 * CGW) { const int r = idx / CGW, cc = idx - r * CGW; fin[idx] = get_granule(sl + (size_t)r * DM + j * CGW + cc); }
  }
  __syncthreads();
  if (tid < CGW) {
    float s = 0.f, mo = 0.f;
#pragma unroll
    for (int r = 0; r < NH; ++r) s += fin[r * CGW + tid];
#pragma unroll
    for (int r = NH; r < 40; ++r) mo += fin[r * CGW + tid];
    const int c = j * CGW + tid;
    const float sb = rbf(s), mb = rbf(mo);
    const float gg = rbf(mb / (1.f + __expf(-mb)));
    out[c] = f2bf(rbf(bf2f(x[c]) + rbf(sb * gg)));
  }
}

extern "C" void kernel_launch(void* const* d_in, const int* in_sizes, int n_in,
                              void* d_out, int out_size, void* d_ws, size_t ws_size,
                              hipStream_t stream) {
  const uint16_t* x = (const uint16_t*)d_in[0];
  const uint16_t* kc = (const uint16_t*)d_in[1];
  const uint16_t* vc = (const uint16_t*)d_in[2];
  const uint16_t* wq = (const uint16_t*)d_in[3];
  const uint16_t* wo = (const uint16_t*)d_in[4];
  const uint16_t* w1 = (const uint16_t*)d_in[6];
  const uint16_t* w3 = (const uint16_t*)d_in[7];
  const uint16_t* w2 = (const uint16_t*)d_in[8];
  char* ws = (char*)d_ws;
  float* qp = (float*)(ws + WS_QP);
  float* gp = (float*)(ws + WS_GP);
  unsigned long long* pg = (unsigned long long*)(ws + WS_PG);
  unsigned long long* sl = (unsigned long long*)(ws + WS_SL);
  k1_proj<<<dim3(96 + 48, RS), dim3(256), 0, stream>>>(x, wq, w1, w3, qp, gp, (u32x4*)(ws + WS_PG));
  k2_attn<<<dim3(NATT + NGLU), dim3(NW * 64), 0, stream>>>(qp, gp, kc, vc, wo, w2, pg, sl, x, (uint16_t*)d_out);
}
```
